```python
import torch

def _softmax(x, axis=-1):
    return torch.softmax(x, dim=axis)

def _gelu(x):
    return torch.nn.functional.gelu(x, approximate='tanh')

def _silu(x):
    return torch.nn.functional.silu(x)

def _sigmoid(x):
    return torch.sigmoid(x)

def _layernorm(x, axis=-1, eps=1e-05):
    m = x.mean(dim=axis, keepdim=True)
    v = x.var(dim=axis, keepdim=True, unbiased=False)
    return (x - m) / torch.sqrt(v + eps)

def _scatter_add(tgt, idx, src, axis=0):
    return tgt.clone().index_add_(axis, idx, src)

def _segment_sum(src, seg, n):
    out = torch.zeros((n,) + src.shape[1:], dtype=src.dtype)
    return out.index_add_(0, seg, src)

def _pad(x, pw):
    flat = tuple((v for pair in reversed(pw) for v in pair))
    return torch.nn.functional.pad(x, flat)
kernel_name = 'adaLN_cond__sliding_block_cached__glu_gated_bilinear__decode'

def setup_inputs():

    _w = lambda a: (lambda t: t.to(torch.bfloat16) if t.is_floating_point() else t)(torch.as_tensor(a))

    def _A_setup():
        B, Sq, D = (1, 1, 640)
        Hq, Hkv, Sk, Dh = (32, 32, 32768, 192)
        assert Hq % Hkv == 0, (Hq, Hkv)
        return {'x': _w((lambda *s: torch.randn(*s, dtype=torch.bfloat16) * 0.05)(B, Sq, D)), 'k_cache': _w((lambda *s: torch.randn(*s, dtype=torch.bfloat16) * 3.2)(B, Hkv, Sk, Dh)), 'v_cache': _w((lambda *s: torch.randn(*s, dtype=torch.bfloat16) * 3.2)(B, Hkv, Sk, Dh)), 'wq': _w((lambda *s: torch.randn(*s, dtype=torch.bfloat16) * 3.2)(D, Hq * Dh) * D ** (-0.5)), 'wo': _w((lambda *s: torch.randn(*s, dtype=torch.bfloat16) * 3.2)(Hq * Dh, D) * (Hq * Dh) ** (-0.5))}
    _A_in = _A_setup()

    def _B_setup():
        B, S, D, F = (1, 1, 640, 1536)
        return {'x': _w((lambda *s: torch.randn(*s, dtype=torch.bfloat16) * 0.05)(B, S, D)), 'w_gate': _w((lambda *s: torch.randn(*s, dtype=torch.bfloat16) * 3.2)(D, F) * D ** (-0.5)), 'w_up': _w((lambda *s: torch.randn(*s, dtype=torch.bfloat16) * 3.2)(D, F) * D ** (-0.5)), 'w_down': _w((lambda *s: torch.randn(*s, dtype=torch.bfloat16) * 3.2)(F, D) * F ** (-0.5))}
    _B_in = _B_setup()
    return {'A_x': _A_in['x'], 'A_k_cache': _A_in['k_cache'], 'A_v_cache': _A_in['v_cache'], 'A_wq': _A_in['wq'], 'A_wo': _A_in['wo'], 'B_x': _B_in['x'], 'B_w_gate': _B_in['w_gate'], 'B_w_up': _B_in['w_up'], 'B_w_down': _B_in['w_down'], 'c': _w((lambda *s: torch.randn(*s, dtype=torch.bfloat16) * 0.2)(1, 640) * 8.0), 'w_mod': _w((lambda *s: torch.randn(*s, dtype=torch.bfloat16) * 0.2)(640, 6 * 640) * 640 ** (-0.5) * 8.0)}

def reference(A_x, A_k_cache, A_v_cache, A_wq, A_wo, B_x, B_w_gate, B_w_up, B_w_down, c, w_mod):

    def _A_ref(x, k_cache, v_cache, wq, wo):
        B, Sq, D = x.shape
        Hq, Dh = (32, 192)
        Hkv, Sk = (k_cache.shape[1], k_cache.shape[2])
        W = min(4096, max(256, Sk // 4))
        rep = Hq // Hkv
        q = (lambda x, *d: x.permute(*d))((x @ wq).reshape(B, Sq, Hq, Dh), 0, 2, 1, 3)
        k = (lambda x, n, axis: torch.repeat_interleave(x, n, dim=axis))(k_cache[:, :, Sk - W:, :], rep, axis=1)
        v = (lambda x, n, axis: torch.repeat_interleave(x, n, dim=axis))(v_cache[:, :, Sk - W:, :], rep, axis=1)
        scores = torch.einsum('bhqd,bhkd->bhqk', q, k) * Dh ** (-0.5)
        p = _softmax(scores, axis=-1)
        o = torch.einsum('bhqk,bhkd->bhqd', p, v)
        return (lambda x, *d: x.permute(*d))(o, 0, 2, 1, 3).reshape(B, Sq, Hq * Dh) @ wo

    def _B_ref(x, w_gate, w_up, w_down):
        g = torch.einsum('bsd,df->bsf', x, w_gate)
        u = torch.einsum('bsd,df->bsf', x, w_up)
        h = g * u
        return torch.einsum('bsf,fd->bsd', h, w_down)
    x = A_x
    mod = (_silu(c) @ w_mod).reshape(c.shape[0], 6, 1, -1)
    sa, ca, ga, sb, cb, gb = (mod[:, 0].clone(), mod[:, 1].clone(), mod[:, 2].clone(), mod[:, 3].clone(), mod[:, 4].clone(), mod[:, 5].clone())
    h = _layernorm(x, axis=-1) * (1 + ca) + sa
    x = x + ga * _A_ref(h, A_k_cache, A_v_cache, A_wq, A_wo)
    h = _layernorm(x, axis=-1) * (1 + cb) + sb
    x = x + gb * _B_ref(h, B_w_gate, B_w_up, B_w_down)
    return x
```

```cpp
#include <hip/hip_runtime.h>
#include <hip/hip_bf16.h>
#include <stdint.h>
#define MIMIC 0
typedef unsigned short u16;
__device__ __forceinline__ float b2f(u16 v) { return __uint_as_float(((unsigned)v) << 16); }
__device__ __forceinline__ float rb(float x) {
  return x;
}
__device__ float block_sum(float v, float* red) {
  for (int o = 32; o; o >>= 1) v += __shfl_xor(v, o);
  __syncthreads();
  if ((threadIdx.x & 63) == 0) red[threadIdx.x >> 6] = v;
  __syncthreads();
  float s = 0; for (int i = 0; i < (int)(blockDim.x >> 6); ++i) s += red[i];
  return s;
}
__global__ void gemv(const float* x, const u16* W, float* y, int K, int N) {
  __shared__ float part[4][64];
  int n = blockIdx.x * 64 + (threadIdx.x & 63), w = threadIdx.x >> 6;
  float a = 0;
  for (int k = w; k < K; k += 4) a += x[k] * b2f(W[(size_t)k * N + n]);
  part[w][threadIdx.x & 63] = a;
  __syncthreads();
  if (w == 0) { int l = threadIdx.x; y[n] = rb(part[0][l] + part[1][l] + part[2][l] + part[3][l]); }
}
__global__ void silu_k(const u16* c, float* s) { int i = threadIdx.x; float v = b2f(c[i]); s[i] = rb(v / (1.f + __expf(-v))); }
__global__ void ln_k(const u16* xb, const float* xf, const float* add, const float* g, float* xo, const float* ss, const float* cc, float* h) {
  __shared__ float red[16];
  int i = threadIdx.x;
  float x = xb ? b2f(xb[i]) : xf[i];
  if (add) { x = rb(x + rb(g[i] * add[i])); xo[i] = x; }
  float m = block_sum(x, red) / 640.f;
  float d = x - m;
  float v = rb(block_sum(d * d, red) / 640.f);
  m = rb(m);
  float n = rb(rb(x - m) / rb(sqrtf(rb(v + 1e-5f))));
  h[i] = rb(rb(n * rb(1.f + cc[i])) + ss[i]);
}
__global__ void attn_k(const float* q, const u16* K, const u16* V, float* o) {
  __shared__ float sc[4096]; __shared__ float red[16]; __shared__ float qs[192];
  int h = blockIdx.x, t = threadIdx.x;
  if (t < 192) qs[t] = q[h * 192 + t];
  __syncthreads();
  const u16* kb = K + ((size_t)h * 32768 + 28672) * 192;
  const u16* vb = V + ((size_t)h * 32768 + 28672) * 192;
  float mx = -INFINITY;
  for (int k = t; k < 4096; k += 1024) { float a = 0; for (int d = 0; d < 192; ++d) a += qs[d] * b2f(kb[(size_t)k * 192 + d]);
    a = rb(rb(a) * 0.07216878364870322f); sc[k] = a; mx = fmaxf(mx, a); }
  for (int o2 = 32; o2; o2 >>= 1) mx = fmaxf(mx, __shfl_xor(mx, o2));
  if ((t & 63) == 0) red[t >> 6] = mx;
  __syncthreads();
  mx = red[0]; for (int i = 1; i < 16; ++i) mx = fmaxf(mx, red[i]);
  float s = 0;
  for (int k = t; k < 4096; k += 1024) { float e = __expf(sc[k] - mx); sc[k] = e; s += e; }
  s = block_sum(s, red);
  for (int k = t; k < 4096; k += 1024) sc[k] = rb(sc[k] / s);
  __syncthreads();
  if (t < 192) { float a = 0; for (int k = 0; k < 4096; ++k) a += sc[k] * b2f(vb[(size_t)k * 192 + t]); o[h * 192 + t] = rb(a); }
}
__global__ void mul_k(const float* g, const float* u, float* hh) { int i = blockIdx.x * 256 + threadIdx.x; hh[i] = rb(g[i] * u[i]); }
__global__ void out_k(const float* x1, const float* gb, const float* d, __hip_bfloat16* out) { int i = threadIdx.x; out[i] = __float2bfloat16(x1[i] + rb(gb[i] * d[i])); }
extern "C" void kernel_launch(void* const* d_in, const int* in_sizes, int n_in, void* d_out, int out_size, void* d_ws, size_t ws_size, hipStream_t stream) {
  const u16 *x = (const u16*)d_in[0], *K = (const u16*)d_in[1], *V = (const u16*)d_in[2], *wq = (const u16*)d_in[3], *wo = (const u16*)d_in[4];
  const u16 *wg = (const u16*)d_in[6], *wu = (const u16*)d_in[7], *wd = (const u16*)d_in[8], *c = (const u16*)d_in[9], *wm = (const u16*)d_in[10];
  float* ws = (float*)d_ws;
  float *s = ws, *mod = ws + 1024, *h = ws + 8192, *q = ws + 16384, *o = ws + 24576, *at = ws + 32768, *x1 = ws + 34816, *h2 = ws + 36864, *g = ws + 40960, *u = ws + 45056, *hh = ws + 49152, *dn = ws + 53248;
  silu_k<<<1, 640, 0, stream>>>(c, s);
  gemv<<<60, 256, 0, stream>>>(s, wm, mod, 640, 3840);
  ln_k<<<1, 640, 0, stream>>>(x, nullptr, nullptr, nullptr, nullptr, mod, mod + 640, h);
  gemv<<<96, 256, 0, stream>>>(h, wq, q, 640, 6144);
  attn_k<<<32, 1024, 0, stream>>>(q, K, V, o);
  gemv<<<10, 256, 0, stream>>>(o, wo, at, 6144, 640);
  ln_k<<<1, 640, 0, stream>>>(x, nullptr, at, mod + 1280, x1, mod + 1920, mod + 2560, h2);
  gemv<<<24, 256, 0, stream>>>(h2, wg, g, 640, 1536);
  gemv<<<24, 256, 0, stream>>>(h2, wu, u, 640, 1536);
  mul_k<<<6, 256, 0, stream>>>(g, u, hh);
  gemv<<<10, 256, 0, stream>>>(hh, wd, dn, 1536, 640);
  out_k<<<1, 640, 0, stream>>>(x1, mod + 3200, dn, (__hip_bfloat16*)d_out);
}
```

```cpp
#include <hip/hip_runtime.h>
#include <hip/hip_bf16.h>
#include <stdint.h>

typedef unsigned u32;
typedef unsigned long long u64;
typedef unsigned short u16;
typedef u32 u32x4 __attribute__((ext_vector_type(4)));
typedef u32 u32x2 __attribute__((ext_vector_type(2)));
typedef float f32x4 __attribute__((ext_vector_type(4)));
typedef float f32x2 __attribute__((ext_vector_type(2)));
typedef __bf16 bf16x2 __attribute__((ext_vector_type(2)));

#define PACE 6
#define PRE 3
#define STAMP 0

constexpr int DM = 640, NH = 32, DH = 192, SK = 32768, WIN = 4096, FF = 1536;
constexpr int NWG = 256, NTHR = 768;
constexpr u32 WS_E = 0;
constexpr u32 WS_MOD = 4096;
constexpr u32 WS_ML = WS_MOD + 3840 * 8;
constexpr u32 WS_X1 = WS_ML + 256 * 2 * 8;
constexpr u32 WS_HH = WS_X1 + 640 * 8;
constexpr u32 WS_QP = 65536;
constexpr u32 WS_G1 = WS_QP + 256 * 192 * 8;
constexpr u32 WS_G2 = WS_G1 + 256 * 192 * 8;
constexpr u32 WS_END = WS_G2 + 256 * 640 * 8;
constexpr u32 WS_STAMP = 4u << 20;
static_assert(WS_HH + 1536 * 8 <= WS_QP, "ws map");
constexpr u32 SPIN_CAP = 1u << 17;

#define RSRC(p, n) __builtin_amdgcn_make_buffer_rsrc((void*)(p), 0, (n), 0x00020000)
typedef __amdgpu_buffer_rsrc_t rsrc_t;
__device__ __forceinline__ u32x4 ld16nt(rsrc_t r, u32 v, u32 s) { return __builtin_amdgcn_raw_buffer_load_b128(r, v, s, 2); }
__device__ __forceinline__ u32x4 ld16(rsrc_t r, u32 v, u32 s) { return __builtin_amdgcn_raw_buffer_load_b128(r, v, s, 0); }
__device__ __forceinline__ u32 ld4nt(rsrc_t r, u32 v, u32 s) { return __builtin_amdgcn_raw_buffer_load_b32(r, v, s, 2); }
__device__ __forceinline__ u32 ld4(rsrc_t r, u32 v, u32 s) { return __builtin_amdgcn_raw_buffer_load_b32(r, v, s, 0); }
__device__ __forceinline__ u32 ld2(rsrc_t r, u32 v, u32 s) { return (u32)__builtin_amdgcn_raw_buffer_load_b16(r, v, s, 0); }
__device__ __forceinline__ u32x4 ld16c(rsrc_t r, u32 v, u32 s) { return __builtin_amdgcn_raw_buffer_load_b128(r, v, s, 16); }
__device__ __forceinline__ u32x2 ld8c(rsrc_t r, u32 v, u32 s) { return __builtin_amdgcn_raw_buffer_load_b64(r, v, s, 16); }
__device__ __forceinline__ void st16c(u32x4 x, rsrc_t r, u32 v, u32 s) { __builtin_amdgcn_raw_buffer_store_b128(x, r, v, s, 16); }
__device__ __forceinline__ void st8c(u32x2 x, rsrc_t r, u32 v, u32 s) { __builtin_amdgcn_raw_buffer_store_b64(x, r, v, s, 16); }

__device__ __forceinline__ float asf(u32 x) { return __builtin_bit_cast(float, x); }
__device__ __forceinline__ u32 asu(float x) { return __builtin_bit_cast(u32, x); }
__device__ __forceinline__ u32 bfbits(float x) { return (u32)__builtin_bit_cast(u16, (__bf16)x); }
__device__ __forceinline__ float rb(float x) { return asf(bfbits(x) << 16); }
__device__ __forceinline__ float blo(u32 x) { return asf(x << 16); }
__device__ __forceinline__ float bhi(u32 x) { return asf(x & 0xffff0000u); }
__device__ __forceinline__ float dot2(u32 a, u32 b, float c) {
  return __builtin_amdgcn_fdot2_f32_bf16(__builtin_bit_cast(bf16x2, a), __builtin_bit_cast(bf16x2, b), c, false);
}
template <int CTRL> __device__ __forceinline__ float dpp(float x) {
  return asf((u32)__builtin_amdgcn_mov_dpp((int)asu(x), CTRL, 0xf, 0xf, true));
}
constexpr int XOR1 = 0xB1, XOR2 = 0x4E, HMIR = 0x141, ROR4 = 0x124, ROR8 = 0x128;
__device__ __forceinline__ float sw32(float a, float b) {
  auto r = __builtin_amdgcn_permlane32_swap(asu(a), asu(b), false, false);
  return asf(r[0]) + asf(r[1]);
}
__device__ __forceinline__ float sw16(float a, float b) {
  auto r = __builtin_amdgcn_permlane16_swap(asu(a), asu(b), false, false);
  return asf(r[0]) + asf(r[1]);
}
__device__ __forceinline__ float mx32(float a) {
  auto r = __builtin_amdgcn_permlane32_swap(asu(a), asu(a), false, false);
  return fmaxf(asf(r[0]), asf(r[1]));
}
__device__ __forceinline__ float mx16(float a) {
  auto r = __builtin_amdgcn_permlane16_swap(asu(a), asu(a), false, false);
  return fmaxf(asf(r[0]), asf(r[1]));
}
__device__ __forceinline__ float wave_sum(float v) {
  v += dpp<XOR1>(v); v += dpp<XOR2>(v); v += dpp<HMIR>(v); v += dpp<ROR8>(v);
  v = sw16(v, v); return sw32(v, v);
}
#define LAS __attribute__((address_space(3)))
__device__ __forceinline__ float fdiv(float n, float d, float r) { const float q = n * r; return fmaf(fmaf(-q, d, n), r, q); }
#define FENCE() asm volatile("" ::: "memory")
#define SB() __builtin_amdgcn_sched_barrier(0)
#define BAR() do { SB(); asm volatile("s_waitcnt lgkmcnt(0)\n\ts_barrier" ::: "memory"); SB(); } while (0)
#define VMW(n) do { SB(); asm volatile("s_waitcnt vmcnt(%0)" ::"n"(n) : "memory"); SB(); } while (0)

__device__ __forceinline__ u32 gpack(float v, u32 t8) { return ((asu(v) + 0x80u) & 0xffffff00u) | t8; }
__device__ __forceinline__ u32 gbad(u32 lo, u32 hi, u32 t32, u32 t8) { return ((lo ^ t8) & 0xffu) | (hi ^ t32); }
__device__ __forceinline__ float gval(u32 lo) { return asf(lo & 0xffffff00u); }
__device__ __forceinline__ bool all_ok(u32 bad) { return __builtin_amdgcn_ballot_w64(bad != 0u) == 0ull; }

struct Args {
  const u16 *x, *kc, *vc, *wq, *wo, *wg, *wu, *wd, *c, *wm;
  u16* out;
  unsigned char* ws;
};

#define STAMPW(i) do { } while (0)

constexpr int L_FLAG = 0, L_TAG = 2, L_H = 4  , L_Q = 96  , L_WM = 192  , L_O = 208  , L_SC = 256  ,
              L_RED = 768  , L_H2 = 2304  , L_R2 = 2944  , L_END = 3072;

__global__ void __launch_bounds__(NTHR, 3) fused_kernel(Args a) {
  __shared__ __attribute__((aligned(16))) float lds[L_END];
  LAS u32* ldsu = (LAS u32*)lds;
  volatile LAS u32* ldsv = (volatile LAS u32*)lds;
  const int tid = threadIdx.x, lane = tid & 63;
  const int wave = __builtin_amdgcn_readfirstlane(tid >> 6);
  const int b = blockIdx.x, xc = b & 7, y = b >> 3;
  const int h = xc * 4 + (y >> 3), s = y & 7;
  const int k8 = lane >> 3, c8 = lane & 7;
  const rsrc_t rws = RSRC(a.ws, WS_END);
  const bool is_gu = y < 12;
  const int gi = xc * 12 + y;
  const int ri = xc * 20 + (y - 12);
  const bool is_dn = (y >= 12) && (y < 22);
  const int di = xc * 10 + (y - 12);

  if (wave == 8 && lane == 0) ldsv[L_FLAG] = 0u;
  if (wave == 8) STAMPW(0);
  BAR();

  if (wave < 8) {
    const size_t kvoff = ((size_t)h * SK + (SK - WIN) + s * 512 + wave * 64) * DH;
    const rsrc_t rk = RSRC(a.kc + kvoff, 64 * DH * 2), rv = RSRC(a.vc + kvoff, 64 * DH * 2);
    const u32 voff = k8 * 384 + c8 * 16;
    u32x4 K[8][3], V[8][3];
#pragma unroll
    for (int t = 0; t < 8; ++t) {
#pragma unroll
      for (int j = 0; j < 3; ++j) K[t][j] = ld16nt(rk, voff + j * 128, t * 3072);
      VMW(PACE);
    }
#pragma unroll
    for (int t = 0; t < PRE; ++t) {
#pragma unroll
      for (int j = 0; j < 3; ++j) V[t][j] = ld16nt(rv, voff + j * 128, t * 3072);
      if (t + 1 < PRE) VMW(PACE);
    }
    if (wave == 0) STAMPW(8);
    BAR();
    if (wave == 0) STAMPW(9);
    u32x4 q[3];
#pragma unroll
    for (int j = 0; j < 3; ++j) q[j] = *(const u32x4*)&lds[L_Q + j * 32 + c8 * 4];
    const u32 t32 = ldsu[L_TAG], t8 = ldsu[L_TAG + 1];
    float sc[8];
#pragma unroll
    for (int t = 0; t < 8; ++t) {
      float d0 = 0.f, d1 = 0.f;
#pragma unroll
      for (int j = 0; j < 3; ++j) {
        d0 = dot2(K[t][j][0], q[j][0], d0); d1 = dot2(K[t][j][1], q[j][1], d1);
        d0 = dot2(K[t][j][2], q[j][2], d0); d1 = dot2(K[t][j][3], q[j][3], d1);
      }
      float d = d0 + d1;
      d += dpp<XOR1>(d); d += dpp<XOR2>(d); d += dpp<HMIR>(d);
      sc[t] = rb(rb(d) * 0.07216878364870322f);
      SB();
      if (PRE + t < 8) {
#pragma unroll
        for (int j = 0; j < 3; ++j) V[PRE + t][j] = ld16nt(rv, voff + j * 128, (PRE + t) * 3072);
      }
      SB();
    }
    u32 wo[24];
    {
      const rsrc_t rw = RSRC(a.wo + (size_t)(h * DH + s * 24) * DM, 24 * DM * 2);
      const u32 wov = tid < 320 ? tid * 4 : 0x40000000u;
#pragma unroll
      for (int r = 0; r < 24; ++r) wo[r] = ld4nt(rw, wov, r * 1280);
    }
    SB();
    float mw = sc[0];
#pragma unroll
    for (int t = 1; t < 8; ++t) mw = fmaxf(mw, sc[t]);
    mw = fmaxf(mw, dpp<ROR8>(mw)); mw = mx16(mw); mw = mx32(mw);
    if (c8 == 0) {
      *(f32x4*)&lds[L_SC + wave * 64 + k8 * 8] = f32x4{sc[0], sc[1], sc[2], sc[3]};
      *(f32x4*)&lds[L_SC + wave * 64 + k8 * 8 + 4] = f32x4{sc[4], sc[5], sc[6], sc[7]};
    }
    if (lane == 0) lds[L_WM + wave] = mw;
    BAR();
    if (wave == 0) STAMPW(10);
    {
      const f32x4 m0 = *(const f32x4*)&lds[L_WM], m1 = *(const f32x4*)&lds[L_WM + 4];
      mw = fmaxf(fmaxf(fmaxf(m0[0], m0[1]), fmaxf(m0[2], m0[3])), fmaxf(fmaxf(m1[0], m1[1]), fmaxf(m1[2], m1[3])));
    }
    u32 pb[8];
#pragma unroll
    for (int t = 0; t < 8; ++t) pb[t] = bfbits(__expf(sc[t] - mw));
    float acc[24];
#pragma unroll
    for (int i = 0; i < 24; ++i) acc[i] = 0.f;
#pragma unroll
    for (int t = 0; t < 8; ++t) {
      const u32 pl = pb[t], ph = pb[t] << 16;
#pragma unroll
      for (int j = 0; j < 3; ++j)
#pragma unroll
        for (int e = 0; e < 4; ++e) {
          acc[j * 8 + 2 * e] = dot2(V[t][j][e], pl, acc[j * 8 + 2 * e]);
          acc[j * 8 + 2 * e + 1] = dot2(V[t][j][e], ph, acc[j * 8 + 2 * e + 1]);
        }
      SB();
    }
    if (wave == 0) STAMPW(11);
#pragma unroll
    for (int i = 0; i < 12; ++i) acc[i] = sw32(acc[i], acc[i + 12]);
#pragma unroll
    for (int i = 0; i < 6; ++i) acc[i] = sw16(acc[i], acc[i + 6]);
#pragma unroll
    for (int i = 0; i < 6; ++i) acc[i] += dpp<ROR8>(acc[i]);
    if ((lane & 8) == 0) {
      float* p = &lds[L_RED + wave * 192 + c8 * 24 + (lane >> 5) * 12 + ((lane >> 4) & 1) * 6];
      *(f32x2*)(p) = f32x2{acc[0], acc[1]}; *(f32x2*)(p + 2) = f32x2{acc[2], acc[3]}; *(f32x2*)(p + 4) = f32x2{acc[4], acc[5]};
    }
    BAR();
    u32 wa[12], wb[12];
    if (wave < 5) {
#pragma unroll
      for (int r = 0; r < 12; ++r) {
        wa[r] = __builtin_amdgcn_perm(wo[2 * r + 1], wo[2 * r], 0x05040100u);
        wb[r] = __builtin_amdgcn_perm(wo[2 * r + 1], wo[2 * r], 0x07060302u);
      }
    }
    BAR();
    if (wave == 0) STAMPW(12);
    if (wave < 5) {
      float p0 = 0.f, p1 = 0.f;
#pragma unroll
      for (int r4 = 0; r4 < 3; ++r4) {
        const u32x4 o4 = *(const u32x4*)&lds[L_O + r4 * 4];
#pragma unroll
        for (int e = 0; e < 4; ++e) { p0 = dot2(wa[r4 * 4 + e], o4[e], p0); p1 = dot2(wb[r4 * 4 + e], o4[e], p1); }
      }
      st16c(u32x4{gpack(p0, t8), t32, gpack(p1, t8), t32}, rws, WS_G2 + b * 5120 + tid * 16, 0);
    }
    SB();
    if (is_gu) {
      const rsrc_t rg = RSRC((wave < 4 ? a.wg : a.wu) + (size_t)(wave & 3) * 160 * FF + gi * 16, 160 * FF * 2);
      u32x4 w[5];
#pragma unroll
      for (int i = 0; i < 5; ++i) w[i] = ld16(rg, (lane >> 1) * (FF * 2) + (lane & 1) * 16, i * 32 * FF * 2);
      BAR();
      if (wave == 0) STAMPW(13);
      float g[8];
#pragma unroll
      for (int e = 0; e < 8; ++e) g[e] = 0.f;
#pragma unroll
      for (int i = 0; i < 5; ++i) {
        const u32 hl = ldsu[L_H2 + (wave & 3) * 160 + i * 32 + (lane >> 1)], hh = hl << 16;
#pragma unroll
        for (int e = 0; e < 4; ++e) { g[2 * e] = dot2(w[i][e], hl, g[2 * e]); g[2 * e + 1] = dot2(w[i][e], hh, g[2 * e + 1]); }
      }
#pragma unroll
      for (int e = 0; e < 4; ++e) g[e] = sw32(g[e], g[e + 4]);
#pragma unroll
      for (int e = 0; e < 2; ++e) g[e] = sw16(g[e], g[e + 2]);
#pragma unroll
      for (int e = 0; e < 2; ++e) { g[e] += dpp<XOR2>(g[e]); g[e] += dpp<ROR4>(g[e]); g[e] += dpp<ROR8>(g[e]); }
      if ((lane & 14) == 0) *(f32x2*)&lds[L_R2 + wave * 16 + (lane & 1) * 8 + (lane >> 5) * 4 + ((lane >> 4) & 1) * 2] = f32x2{g[0], g[1]};
      BAR();
    } else {
      float v0, v1;
      for (u32 spins = 0;; ++spins) {
        FENCE();
        const u32x4 g = ld16c(rws, WS_G2 + (tid >> 1) * 5120 + ri * 32 + (tid & 1) * 16, 0);
        v0 = gval(g[0]); v1 = gval(g[2]);
        if (all_ok(gbad(g[0], g[1], t32, t8) | gbad(g[2], g[3], t32, t8)) || spins > SPIN_CAP) break;
      }
      SB();
      u32x4 w[3];
      if (is_dn) {
        const rsrc_t rd = RSRC(a.wd + (size_t)wave * 192 * DM + di * 8, 192 * DM * 2);
#pragma unroll
        for (int i = 0; i < 3; ++i) w[i] = ld16(rd, lane * (DM * 2), i * 64 * DM * 2);
      }
      SB();
      v0 = sw32(v0, v1);
      v0 += dpp<XOR2>(v0); v0 += dpp<ROR4>(v0); v0 += dpp<ROR8>(v0); v0 = sw16(v0, v0);
      if ((lane & 30) == 0) lds[L_R2 + wave * 4 + (lane & 1) * 2 + (lane >> 5)] = v0;
      BAR();
      if (wave == 0) STAMPW(13);
      if (is_dn) {
        u32 hv[3];
        for (u32 spins = 0;; ++spins) {
          FENCE();
          u32 bad = 0;
#pragma unroll
          for (int i = 0; i < 3; ++i) {
            const u32x2 g = ld8c(rws, WS_HH + (wave * 192 + lane) * 8, i * 512);
            hv[i] = g[0] >> 16; bad |= gbad(g[0], g[1], t32, t8);
          }
          if (all_ok(bad) || spins > SPIN_CAP) break;
        }
        if (wave == 0) STAMPW(14);
        float g[8];
#pragma unroll
        for (int e = 0; e < 8; ++e) g[e] = 0.f;
#pragma unroll
        for (int i = 0; i < 3; ++i) {
          const u32 hl = hv[i], hh = hl << 16;
#pragma unroll
          for (int e = 0; e < 4; ++e) { g[2 * e] = dot2(w[i][e], hl, g[2 * e]); g[2 * e + 1] = dot2(w[i][e], hh, g[2 * e + 1]); }
        }
#pragma unroll
        for (int e = 0; e < 4; ++e) g[e] = sw32(g[e], g[e + 4]);
#pragma unroll
        for (int e = 0; e < 2; ++e) g[e] = sw16(g[e], g[e + 2]);
#pragma unroll
        for (int e = 0; e < 2; ++e) { g[e] += dpp<XOR1>(g[e]); g[e] += dpp<XOR2>(g[e]); g[e] += dpp<HMIR>(g[e]); g[e] += dpp<ROR8>(g[e]); }
        if ((lane & 15) == 0) *(f32x2*)&lds[L_R2 + 64 + wave * 8 + (lane >> 5) * 4 + ((lane >> 4) & 1) * 2] = f32x2{g[0], g[1]};
        BAR();
      }
    }
  } else if (wave == 8) {
    const int mg = xc * 8 + (y >> 2), mj = mg * 4 + (y & 3);
    const rsrc_t rc = RSRC(a.c, DM * 2), rx = RSRC(a.x, DM * 2);
    u32 xv[5], cv[5];
#pragma unroll
    for (int i = 0; i < 5; ++i) cv[i] = ld4(rc, lane * 4, i * 256);
    u32x4 w[20];
    {
      const rsrc_t rm = RSRC(a.wm + (mg < 60 ? mj : 0) * 16, mg < 60 ? (DM - 1) * 3840 * 2 + 32 : 0);
#pragma unroll
      for (int i = 0; i < 20; ++i) w[i] = ld16(rm, (lane >> 1) * 7680 + (lane & 1) * 16, i * 32 * 7680);
    }
    const u32x2 ew = ld8c(rws, WS_E, 0);
#pragma unroll
    for (int i = 0; i < 5; ++i) xv[i] = ld4(rx, lane * 4, i * 256);
    const u32 xs0 = ld2(rx, (s * 80 + lane) * 2, 0), xs1 = ld2(rx, (s * 80 + 64 + (lane & 15)) * 2, 0);
    SB();
#pragma unroll
    for (int i = 0; i < 5; ++i) {
      const float c0 = blo(cv[i]), c1 = bhi(cv[i]);
      *(LAS u32x2*)&ldsu[L_H2 + i * 128 + lane * 2] = u32x2{bfbits(c0 / (1.f + __expf(-c0))), bfbits(c1 / (1.f + __expf(-c1)))};
    }
    asm volatile("s_waitcnt lgkmcnt(0)" ::: "memory");
    u64 e1; u32 t32, t8;
    {
      float g[8];
#pragma unroll
      for (int e = 0; e < 8; ++e) g[e] = 0.f;
#pragma unroll
      for (int i = 0; i < 20; ++i) {
        const u32 hl = ldsu[L_H2 + i * 32 + (lane >> 1)], hh = hl << 16;
#pragma unroll
        for (int e = 0; e < 4; ++e) { g[2 * e] = dot2(w[i][e], hl, g[2 * e]); g[2 * e + 1] = dot2(w[i][e], hh, g[2 * e + 1]); }
      }
#pragma unroll
      for (int e = 0; e < 4; ++e) g[e] = sw32(g[e], g[e + 4]);
#pragma unroll
      for (int e = 0; e < 2; ++e) g[e] = sw16(g[e], g[e + 2]);
#pragma unroll
      for (int e = 0; e < 2; ++e) { g[e] += dpp<XOR2>(g[e]); g[e] += dpp<ROR4>(g[e]); g[e] += dpp<ROR8>(g[e]); }
      e1 = (((u64)ew[1] << 32) | ew[0]) + 1ull;
      t32 = __builtin_amdgcn_readfirstlane((u32)e1); t8 = __builtin_amdgcn_readfirstlane((u32)(e1 >> 32) & 0xffu);
      if ((lane & 14) == 0 && mg < 60)
        st16c(u32x4{(bfbits(g[0]) << 16) | t8, t32, (bfbits(g[1]) << 16) | t8, t32}, rws,
              WS_MOD + (mj * 16 + (lane & 1) * 8 + (lane >> 5) * 4 + ((lane >> 4) & 1) * 2) * 8, 0);
    }
    STAMPW(1);
    float mean0, rstd_den0;
    {
      float sm = 0.f;
#pragma unroll
      for (int i = 0; i < 5; ++i) sm += blo(xv[i]) + bhi(xv[i]);
      const float m = wave_sum(sm) * (1.f / 640.f);
      float sv = 0.f;
#pragma unroll
      for (int i = 0; i < 5; ++i) { const float d0 = blo(xv[i]) - m, d1 = bhi(xv[i]) - m; sv += d0 * d0 + d1 * d1; }
      const float v = rb(wave_sum(sv) * (1.f / 640.f));
      mean0 = rb(m); rstd_den0 = rb(sqrtf(rb(v + 1e-5f)));
    }
    {
      const float n0 = rb(rb(blo(xs0) - mean0) / rstd_den0), n1 = rb(rb(blo(xs1) - mean0) / rstd_den0);
      u32x2 sa0, ca0, sa1, ca1;
      for (u32 spins = 0;; ++spins) {
        FENCE();
        sa0 = ld8c(rws, WS_MOD + (s * 80 + lane) * 8, 0);
        ca0 = ld8c(rws, WS_MOD + (s * 80 + lane) * 8, 640 * 8);
        sa1 = ld8c(rws, WS_MOD + (s * 80 + 64 + (lane & 15)) * 8, 0);
        ca1 = ld8c(rws, WS_MOD + (s * 80 + 64 + (lane & 15)) * 8, 640 * 8);
        const u32 bad = gbad(sa0[0], sa0[1], t32, t8) | gbad(ca0[0], ca0[1], t32, t8) | gbad(sa1[0], sa1[1], t32, t8) | gbad(ca1[0], ca1[1], t32, t8);
        if (all_ok(bad) || spins > SPIN_CAP) break;
        __builtin_amdgcn_s_sleep(2);
      }
      const u32 h0 = bfbits(rb(n0 * rb(1.f + bhi(ca0[0]))) + bhi(sa0[0]));
      const u32 h1 = bfbits(rb(n1 * rb(1.f + bhi(ca1[0]))) + bhi(sa1[0]));
      ldsu[L_H + lane] = h0;
      if (lane < 16) ldsu[L_H + 64 + lane] = h1;
      if (lane == 0) { ldsu[L_TAG] = t32; ldsu[L_TAG + 1] = t8; }
      asm volatile("s_waitcnt lgkmcnt(0)" ::: "memory");
      if (lane == 0) ldsv[L_FLAG] = 1u;
    }
    STAMPW(2);
    u32 sbv[10], cbv[10];
    float gav = 0.f, xrv = 0.f, gbv = 0.f;
    if (is_gu) {
      for (u32 spins = 0;; ++spins) {
        FENCE();
        u32 bad = 0;
#pragma unroll
        for (int i = 0; i < 5; ++i) {
          const u32x4 g0 = ld16c(rws, WS_MOD + 1920 * 8 + lane * 16, i * 1024), g1 = ld16c(rws, WS_MOD + 2560 * 8 + lane * 16, i * 1024);
          sbv[2 * i] = g0[0]; sbv[2 * i + 1] = g0[2]; cbv[2 * i] = g1[0]; cbv[2 * i + 1] = g1[2];
          bad |= gbad(g0[0], g0[1], t32, t8) | gbad(g0[2], g0[3], t32, t8) | gbad(g1[0], g1[1], t32, t8) | gbad(g1[2], g1[3], t32, t8);
        }
        if (all_ok(bad) || spins > SPIN_CAP) break;
        __builtin_amdgcn_s_sleep(2);
      }
    } else {
      xrv = blo(ld2(rx, (ri * 4 + (lane & 3)) * 2, 0));
      for (u32 spins = 0;; ++spins) {
        FENCE();
        const u32x2 g0 = ld8c(rws, WS_MOD + (1280 + ri * 4 + (lane & 3)) * 8, 0);
        const u32x2 g1 = ld8c(rws, WS_MOD + (3200 + (is_dn ? di : 0) * 8 + (lane & 7)) * 8, 0);
        gav = bhi(g0[0]); gbv = bhi(g1[0]);
        if (all_ok(gbad(g0[0], g0[1], t32, t8) | gbad(g1[0], g1[1], t32, t8)) || spins > SPIN_CAP) break;
        __builtin_amdgcn_s_sleep(2);
      }
    }
    BAR();
    BAR();
    float wsc;
    {
      const f32x4 m0 = *(const f32x4*)&lds[L_WM], m1 = *(const f32x4*)&lds[L_WM + 4];
      const float mw = fmaxf(fmaxf(fmaxf(m0[0], m0[1]), fmaxf(m0[2], m0[3])), fmaxf(fmaxf(m1[0], m1[1]), fmaxf(m1[2], m1[3])));
      const f32x4 s0 = *(const f32x4*)&lds[L_SC + lane * 8], s1 = *(const f32x4*)&lds[L_SC + lane * 8 + 4];
      float l = 0.f;
#pragma unroll
      for (int e = 0; e < 4; ++e) l += __expf(s0[e] - mw) + __expf(s1[e] - mw);
      l = wave_sum(l);
      if (lane == 0) st16c(u32x4{(asu(mw) & 0xffffff00u) | t8, t32, gpack(l, t8), t32}, rws, WS_ML + (h * 8 + s) * 16, 0);
      u32x4 g;
      for (u32 spins = 0;; ++spins) {
        FENCE();
        g = ld16c(rws, WS_ML + (h * 8 + c8) * 16, 0);
        if (all_ok(gbad(g[0], g[1], t32, t8) | gbad(g[2], g[3], t32, t8)) || spins > SPIN_CAP) break;
        __builtin_amdgcn_s_sleep(2);
      }
      const float ms = gval(g[0]), ls = gval(g[2]);
      float M = fmaxf(ms, dpp<XOR1>(ms)); M = fmaxf(M, dpp<XOR2>(M)); M = fmaxf(M, dpp<HMIR>(M));
      const float ev = __expf(ms - M);
      float L = ev * ls; L += dpp<XOR1>(L); L += dpp<XOR2>(L); L += dpp<HMIR>(L);
      wsc = ev / L;
    }
    STAMPW(3);
    BAR();
    {
      float o[3];
      for (u32 spins = 0;; ++spins) {
        FENCE();
        u32 bad = 0;
#pragma unroll
        for (int i = 0; i < 3; ++i) {
          const u32x2 g = ld8c(rws, WS_G1 + ((h * 8 + s) * 192 + c8 * 24 + k8) * 8, i * 64);
          o[i] = gval(g[0]); bad |= gbad(g[0], g[1], t32, t8);
        }
        if (all_ok(bad) || spins > SPIN_CAP) break;
      }
#pragma unroll
      for (int i = 0; i < 3; ++i) {
        float v = o[i] * wsc;
        v += dpp<XOR1>(v); v += dpp<XOR2>(v); v += dpp<HMIR>(v);
        if (c8 == 0) ((LAS u16*)&ldsu[L_O])[i * 8 + k8] = (u16)bfbits(v);
      }
    }
    STAMPW(4);
    BAR();
    if (is_gu) {
      float xq[10];
      for (u32 spins = 0;; ++spins) {
        FENCE();
        u32 bad = 0;
#pragma unroll
        for (int i = 0; i < 5; ++i) {
          const u32x4 g = ld16c(rws, WS_X1 + lane * 16, i * 1024);
          xq[2 * i] = bhi(g[0]); xq[2 * i + 1] = bhi(g[2]);
          bad |= gbad(g[0], g[1], t32, t8) | gbad(g[2], g[3], t32, t8);
        }
        if (all_ok(bad) || spins > SPIN_CAP) break;
      }
      STAMPW(5);
      float sm = 0.f;
#pragma unroll
      for (int i = 0; i < 10; ++i) sm += xq[i];
      const float m = wave_sum(sm) * (1.f / 640.f);
      float sv = 0.f;
#pragma unroll
      for (int i = 0; i < 10; ++i) { const float d = xq[i] - m; sv += d * d; }
      const float v = rb(wave_sum(sv) * (1.f / 640.f));
      const float mb = rb(m), den = rb(sqrtf(rb(v + 1e-5f))), rden = __builtin_amdgcn_rcpf(den);
#pragma unroll
      for (int i = 0; i < 5; ++i) {
        u32x2 hv;
#pragma unroll
        for (int e = 0; e < 2; ++e) {
          const float n = rb(fdiv(rb(xq[2 * i + e] - mb), den, rden));
          hv[e] = bfbits(rb(n * rb(1.f + bhi(cbv[2 * i + e]))) + bhi(sbv[2 * i + e]));
        }
        *(u32x2*)&lds[L_H2 + i * 128 + lane * 2] = hv;
      }
      BAR();
      BAR();
      if (lane < 16) {
        const float g = (lds[L_R2 + lane] + lds[L_R2 + 16 + lane]) + (lds[L_R2 + 32 + lane] + lds[L_R2 + 48 + lane]);
        const float u = (lds[L_R2 + 64 + lane] + lds[L_R2 + 80 + lane]) + (lds[L_R2 + 96 + lane] + lds[L_R2 + 112 + lane]);
        st8c(u32x2{(bfbits(rb(g) * rb(u)) << 16) | t8, t32}, rws, WS_HH + (gi * 16 + lane) * 8, 0);
      }
      STAMPW(6);
    } else {
      BAR();
      if (lane < 4) {
        float at = 0.f;
#pragma unroll
        for (int w = 0; w < 8; ++w) at += lds[L_R2 + w * 4 + lane];
        const u32 x1 = bfbits(xrv + rb(gav * rb(at)));
        st8c(u32x2{(x1 << 16) | t8, t32}, rws, WS_X1 + (ri * 4 + lane) * 8, 0);
      }
      STAMPW(5);
      if (is_dn) {
        float x1v;
        for (u32 spins = 0;; ++spins) {
          FENCE();
          const u32x2 g = ld8c(rws, WS_X1 + (di * 8 + c8) * 8, 0);
          x1v = bhi(g[0]);
          if (all_ok(gbad(g[0], g[1], t32, t8)) || spins > SPIN_CAP) break;
          __builtin_amdgcn_s_sleep(1);
        }
        BAR();
        if (lane < 8) {
          float dn = 0.f;
#pragma unroll
          for (int w = 0; w < 8; ++w) dn += lds[L_R2 + 64 + w * 8 + lane];
          a.out[di * 8 + lane] = (u16)bfbits(x1v + rb(gbv * rb(dn)));
        }
        if (di == 0 && lane == 0) st8c(u32x2{(u32)e1, (u32)(e1 >> 32)}, rws, WS_E, 0);
        STAMPW(6);
      }
    }
  } else {
    const int j = wave - 9;
    const rsrc_t rq = RSRC(a.wq + (size_t)s * 80 * (NH * DH) + h * DH + j * 64, 80 * NH * DH * 2);
    u32x4 w[10];
#pragma unroll
    for (int t = 0; t < 10; ++t) w[t] = ld16nt(rq, k8 * (NH * DH * 2) + c8 * 16, t * 8 * NH * DH * 2);
    SB();
    for (u32 spins = 0; ldsv[L_FLAG] == 0u && spins < (SPIN_CAP << 6); ++spins) __builtin_amdgcn_s_sleep(2);
    FENCE();
    const u32 t32 = ldsu[L_TAG], t8 = ldsu[L_TAG + 1];
    float g[8];
#pragma unroll
    for (int e = 0; e < 8; ++e) g[e] = 0.f;
#pragma unroll
    for (int t = 0; t < 10; ++t) {
      const u32 hl = ldsu[L_H + t * 8 + k8], hh = hl << 16;
#pragma unroll
      for (int e = 0; e < 4; ++e) { g[2 * e] = dot2(w[t][e], hl, g[2 * e]); g[2 * e + 1] = dot2(w[t][e], hh, g[2 * e + 1]); }
    }
#pragma unroll
    for (int e = 0; e < 4; ++e) g[e] = sw32(g[e], g[e + 4]);
#pragma unroll
    for (int e = 0; e < 2; ++e) g[e] = sw16(g[e], g[e + 2]);
#pragma unroll
    for (int e = 0; e < 2; ++e) g[e] += dpp<ROR8>(g[e]);
    if ((lane & 8) == 0)
      st16c(u32x4{gpack(g[0], t8), t32, gpack(g[1], t8), t32}, rws,
            WS_QP + ((h * 8 + s) * 192 + j * 64 + c8 * 8 + (lane >> 5) * 4 + ((lane >> 4) & 1) * 2) * 8, 0);
    float q0, q1;
    for (u32 spins = 0;; ++spins) {
      FENCE();
      u32 bad = 0; q0 = 0.f; q1 = 0.f;
#pragma unroll
      for (int i = 0; i < 4; ++i) {
        const u32x4 gq = ld16c(rws, WS_QP + ((h * 8 + (lane >> 5)) * 192 + j * 64 + (lane & 31) * 2) * 8, i * 2 * 192 * 8);
        q0 += gval(gq[0]); q1 += gval(gq[2]);
        bad |= gbad(gq[0], gq[1], t32, t8) | gbad(gq[2], gq[3], t32, t8);
      }
      if (all_ok(bad) || spins > SPIN_CAP) break;
      __builtin_amdgcn_s_sleep(2);
    }
    q0 = sw32(q0, q0); q1 = sw32(q1, q1);
    if (lane < 32) ldsu[L_Q + j * 32 + lane] = bfbits(q0) | (bfbits(q1) << 16);
    if (wave == 9) STAMPW(16);
    BAR();
    BAR();
    BAR();
    {
      const int u = j * 64 + lane, cc = u / 24, i = u - cc * 24;
      const int d = (i >> 3) * 64 + cc * 8 + (i & 7), sp = d / 24, dd = d - sp * 24;
      float v = 0.f;
#pragma unroll
      for (int w8 = 0; w8 < 8; ++w8) v += lds[L_RED + w8 * 192 + u];
      st8c(u32x2{gpack(v, t8), t32}, rws, WS_G1 + (((h * 8 + sp) * 8 + s) * 24 + dd) * 8, 0);
    }
    if (wave == 9) STAMPW(17);
  }
}

extern "C" void kernel_launch(void* const* d_in, const int* in_sizes, int n_in, void* d_out, int out_size, void* d_ws,
                              size_t ws_size, hipStream_t stream) {
  Args a;
  a.x = (const u16*)d_in[0]; a.kc = (const u16*)d_in[1]; a.vc = (const u16*)d_in[2]; a.wq = (const u16*)d_in[3];
  a.wo = (const u16*)d_in[4]; a.wg = (const u16*)d_in[6]; a.wu = (const u16*)d_in[7]; a.wd = (const u16*)d_in[8];
  a.c = (const u16*)d_in[9]; a.wm = (const u16*)d_in[10];
  a.out = (u16*)d_out; a.ws = (unsigned char*)d_ws;
  fused_kernel<<<NWG, NTHR, 0, stream>>>(a);
}
```

```cpp
#include <hip/hip_runtime.h>
#include <hip/hip_bf16.h>
#include <stdint.h>

typedef unsigned u32;
typedef unsigned long long u64;
typedef unsigned short u16;
typedef u32 u32x4 __attribute__((ext_vector_type(4)));
typedef u32 u32x2 __attribute__((ext_vector_type(2)));
typedef float f32x4 __attribute__((ext_vector_type(4)));
typedef float f32x2 __attribute__((ext_vector_type(2)));
typedef __bf16 bf16x2 __attribute__((ext_vector_type(2)));
typedef __bf16 bf16x8 __attribute__((ext_vector_type(8)));

#define PACE2 9
#define GW 3
#define WSD 1
#define DDN3 90
#define DDN2 8
#define WOE 24
#define KVAUX 2
#define KVPOL "nt"
#define KL 1
#define EB0 1
#define PT 0
#define CH 4
#define WOW 6
#define DX1 30
#define DGU 90
#define WSL 4
#define DDN 10
#define XR 4
#define DE1 12
#define PS 0
#define PRIME 1
#define DMO 100
#define DQ 127
#define SW 1
#define STAMP 0

constexpr int DM = 640, NH = 32, DH = 192, SK = 32768, WIN = 4096, FF = 1536;
constexpr int NWG = 256, NTHR = 768, NRED = 160 / CH, VL = 5;
constexpr u32 WS_E = 0;
constexpr u32 WS_CS = 4096;
constexpr u32 WS_MOD = 16384;
constexpr u32 WS_QP = WS_MOD + 4 * 3840 * 8;
constexpr u32 WS_GU = WS_QP + 256 * 192 * 8;
constexpr u32 WS_G1 = WS_GU + 96 * 3072;
constexpr u32 WS_G5 = WS_G1 + 256 * 3072;
constexpr u32 WS_G2 = WS_G5 + 96 * 2560;
constexpr u32 WS_X1 = WS_G2 + 256 * 2560, X1S = 5376;
constexpr u32 NXR = 16 * XR / CH;
constexpr u32 WS_END = WS_X1 + NXR * X1S;
constexpr u32 WS_STAMP = 4u << 20;
static_assert(WS_CS + 5120 <= WS_MOD && NXR <= 96 && WS_END <= WS_STAMP && CH == 4, "ws map");
constexpr u32 SPIN_CAP = 1u << 17;
constexpr u32 OOB = 0x40000000u;

#define RSRC(p, n) __builtin_amdgcn_make_buffer_rsrc((void*)(p), 0, (n), 0x00020000)
typedef __amdgpu_buffer_rsrc_t rsrc_t;
__device__ __forceinline__ u32x4 ld16nt(rsrc_t r, u32 v, u32 s) { return __builtin_amdgcn_raw_buffer_load_b128(r, v, s, KVAUX); }
__device__ __forceinline__ u32x4 ld16(rsrc_t r, u32 v, u32 s) { return __builtin_amdgcn_raw_buffer_load_b128(r, v, s, 0); }
__device__ __forceinline__ u32x2 ld8nt(rsrc_t r, u32 v, u32 s) { return __builtin_amdgcn_raw_buffer_load_b64(r, v, s, 2); }
__device__ __forceinline__ u32 ld4nt(rsrc_t r, u32 v, u32 s) { return __builtin_amdgcn_raw_buffer_load_b32(r, v, s, 2); }
__device__ __forceinline__ u32 ld4(rsrc_t r, u32 v, u32 s) { return __builtin_amdgcn_raw_buffer_load_b32(r, v, s, 0); }
__device__ __forceinline__ u32 ld2(rsrc_t r, u32 v, u32 s) { return (u32)__builtin_amdgcn_raw_buffer_load_b16(r, v, s, 2); }
__device__ __forceinline__ u32x4 ld16c(rsrc_t r, u32 v, u32 s) { return __builtin_amdgcn_raw_buffer_load_b128(r, v, s, 16); }
__device__ __forceinline__ u32x2 ld8c(rsrc_t r, u32 v, u32 s) { return __builtin_amdgcn_raw_buffer_load_b64(r, v, s, 16); }
__device__ __forceinline__ void st16c(u32x4 x, rsrc_t r, u32 v, u32 s) { __builtin_amdgcn_raw_buffer_store_b128(x, r, v, s, 16); }
__device__ __forceinline__ void st8c(u32x2 x, rsrc_t r, u32 v, u32 s) { __builtin_amdgcn_raw_buffer_store_b64(x, r, v, s, 16); }

__device__ __forceinline__ float asf(u32 x) { return __builtin_bit_cast(float, x); }
__device__ __forceinline__ u32 asu(float x) { return __builtin_bit_cast(u32, x); }
__device__ __forceinline__ u32 bfbits(float x) { return (u32)__builtin_bit_cast(u16, (__bf16)x); }
__device__ __forceinline__ float rb(float x) { return asf(bfbits(x) << 16); }
__device__ __forceinline__ float blo(u32 x) { return asf(x << 16); }
__device__ __forceinline__ float bhi(u32 x) { return asf(x & 0xffff0000u); }
__device__ __forceinline__ float dot2(u32 a, u32 b, float c) {
  return __builtin_amdgcn_fdot2_f32_bf16(__builtin_bit_cast(bf16x2, a), __builtin_bit_cast(bf16x2, b), c, false);
}
template <int CTRL> __device__ __forceinline__ float dpp(float x) {
  return asf((u32)__builtin_amdgcn_mov_dpp((int)asu(x), CTRL, 0xf, 0xf, true));
}
constexpr int XOR1 = 0xB1, XOR2 = 0x4E, HMIR = 0x141, ROR4 = 0x124, ROR8 = 0x128;
__device__ __forceinline__ float sw32(float a, float b) {
  auto r = __builtin_amdgcn_permlane32_swap(asu(a), asu(b), false, false);
  return asf(r[0]) + asf(r[1]);
}
__device__ __forceinline__ float sw16(float a, float b) {
  auto r = __builtin_amdgcn_permlane16_swap(asu(a), asu(b), false, false);
  return asf(r[0]) + asf(r[1]);
}
__device__ __forceinline__ float mx32(float a) {
  auto r = __builtin_amdgcn_permlane32_swap(asu(a), asu(a), false, false);
  return fmaxf(asf(r[0]), asf(r[1]));
}
__device__ __forceinline__ float mx16(float a) {
  auto r = __builtin_amdgcn_permlane16_swap(asu(a), asu(a), false, false);
  return fmaxf(asf(r[0]), asf(r[1]));
}
__device__ __forceinline__ float wave_sum(float v) {
  v += dpp<XOR1>(v); v += dpp<XOR2>(v); v += dpp<HMIR>(v); v += dpp<ROR8>(v);
  v = sw16(v, v); return sw32(v, v);
}
#define LAS __attribute__((address_space(3)))
__device__ __forceinline__ float fdiv(float n, float d, float r) { const float q = n * r; return fmaf(fmaf(-q, d, n), r, q); }
#define FENCE() asm volatile("" ::: "memory")
#define SB() __builtin_amdgcn_sched_barrier(0)
#define BAR() do { SB(); asm volatile("s_waitcnt lgkmcnt(0)\n\ts_barrier" ::: "memory"); SB(); } while (0)
#define VMW(n) do { SB(); asm volatile("s_waitcnt vmcnt(%0)" ::"n"(n) : "memory"); SB(); } while (0)

__device__ __forceinline__ u32 gpack(float v, u32 t8) { return ((asu(v) + 0x80u) & 0xffffff00u) | t8; }
__device__ __forceinline__ u32x4 gpack4(float a, float b, float c, float d, u32 t8) { return u32x4{gpack(a, t8), gpack(b, t8), gpack(c, t8), gpack(d, t8)}; }
__device__ __forceinline__ u32 gbad4(u32x4 g, u32 t8) { return (g[0] ^ t8) | (g[1] ^ t8) | (g[2] ^ t8) | (g[3] ^ t8); }
__device__ __forceinline__ float gval(u32 lo) { return asf(lo & 0xffffff00u); }
__device__ __forceinline__ bool all_ok(u32 bad) { return __builtin_amdgcn_ballot_w64(bad != 0u) == 0ull; }
__device__ __forceinline__ float rows4(float a, float b, float c, float d) { return sw16(sw32(a, c), sw32(b, d)); }

struct Args {
  const u16 *x, *kc, *vc, *wq, *wo, *wg, *wu, *wd, *c, *wm;
  u16* out;
  unsigned char* ws;
};

#define T(k) do { } while (0)
#define TDECL do { } while (0)
#define TDUMP(base) do { } while (0)

constexpr int L_FLAG = 0  , L_TAG = 8, L_H = 16  , L_Q = 96  , L_WM = 192  , L_WL = 200  ,
              L_RED = 768  , L_X = 3840  , L_V = 4096  , L_END = L_V + 8 * VL * 768;

__device__ __forceinline__ f32x4 rowmac(u32x4 w, u32 vv, const u32 (&msk)[4], f32x4 acc) {
  const u32x4 bf = u32x4{vv & msk[0], vv & msk[1], vv & msk[2], vv & msk[3]};
  return __builtin_amdgcn_mfma_f32_16x16x32_bf16(__builtin_bit_cast(bf16x8, w), __builtin_bit_cast(bf16x8, bf), acc, 0, 0, 0);
}
__device__ __forceinline__ f32x4 fold(f32x4 acc) {
  f32x4 r;
#pragma unroll
  for (int i = 0; i < 4; ++i) {
    const float x = dpp<ROR8>(acc[i]);
    auto sw = __builtin_amdgcn_permlane32_swap(asu(x), asu(x), false, false);
    r[i] = acc[i] + asf(sw[1]);
  }
  return r;
}

template <int NP> __device__ __forceinline__ u32x4 proj4(const u32 (&wp)[NP][4], u32 hp, u32 t8) {
  float p[4] = {0.f, 0.f, 0.f, 0.f};
#pragma unroll
  for (int r = 0; r < NP; ++r) {
    const u32 hs = __builtin_amdgcn_readlane(hp, (r & 3) * 16 + (r >> 2) * 2);
#pragma unroll
    for (int e = 0; e < 4; ++e) p[e] = dot2(wp[r][e], hs, p[e]);
  }
  return gpack4(p[0], p[1], p[2], p[3], t8);
}
__device__ __forceinline__ void pair4(u32 (&o)[4], u32x2 lo, u32x2 hi) {
  o[0] = __builtin_amdgcn_perm(hi[0], lo[0], 0x05040100u); o[1] = __builtin_amdgcn_perm(hi[0], lo[0], 0x07060302u);
  o[2] = __builtin_amdgcn_perm(hi[1], lo[1], 0x05040100u); o[3] = __builtin_amdgcn_perm(hi[1], lo[1], 0x07060302u);
}
template <int N, bool W16, int SL> __device__ __forceinline__ void polln(u32x4 (&g)[N], rsrc_t r, const u32 (&off)[N], u32 tg) {
  for (u32 spins = 0; spins < SPIN_CAP; ++spins) {
    u32 bad[N], any = 0;
#pragma unroll
    for (int i = 0; i < N; ++i) { bad[i] = W16 ? gbad4(g[i], tg) & 0xffu : (g[i][1] ^ tg) | (g[i][3] ^ tg); any |= bad[i]; }
    if (all_ok(any)) break;
    if (SL) __builtin_amdgcn_s_sleep(SL);
    FENCE();
#pragma unroll
    for (int i = 0; i < N; ++i) {
      if (SL == 2) {
        const u32x4 n = ld16c(r, bad[i] ? off[i] : OOB, 0);
        const u32 keep = bad[i] ? 0u : ~0u;
        g[i] = (g[i] & keep) | n;
      } else g[i] = ld16c(r, off[i], 0);
    }
  }
}
template <int N, bool W16, int SL> __device__ __forceinline__ void gather(u32x4 (&g)[N], rsrc_t r, const u32 (&off)[N], u32 tg) {
  FENCE();
#pragma unroll
  for (int i = 0; i < N; ++i) g[i] = ld16c(r, off[i], 0);
  polln<N, W16, SL>(g, r, off, tg);
}
__device__ __forceinline__ float redrow(float v) {
  v += dpp<ROR8>(v); v += dpp<ROR4>(v);
  if (CH <= 2) v += dpp<XOR2>(v);
  return CH == 1 ? v + dpp<XOR1>(v) : v;
}

__global__ void __launch_bounds__(NTHR, 3) fused_kernel(Args a) {
  __shared__ __attribute__((aligned(16))) float lds[L_END];
  LAS u32* ldsu = (LAS u32*)lds;
  volatile LAS u32* ldsv = (volatile LAS u32*)lds;
  const int lane = threadIdx.x & 63;
  const int hw = __builtin_amdgcn_readfirstlane(threadIdx.x >> 6);
  const int wave = hw < 4 ? hw + 8 : hw - 4;
  const int b = blockIdx.x, xc = b & 7, y = b >> 3;
  const int h = xc * 4 + (y >> 3), s = y & 7;
  const int k8 = lane >> 3, c8 = lane & 7;
  const rsrc_t rws = RSRC(a.ws, WS_END);
  const bool is_gu = y < 12;
  const int gi = xc * 12 + y;
  const int ri = (31 - y) * 8 + xc;
  const bool is_dn = (y >= 12) && (y < 24);
  const int dj = xc * 12 + (y - 12);
  u32 msk[4];
#pragma unroll
  for (int r = 0; r < 4; ++r) msk[r] = (c8 >> 1) == r ? ((c8 & 1) ? 0xffff0000u : 0x0000ffffu) : 0u;
  const f32x4 Z4 = f32x4{0.f, 0.f, 0.f, 0.f};

  TDECL;
#define LWAIT(c, n) do { for (u32 sp_ = 0; !(c) && sp_ < (SPIN_CAP << 6); ++sp_) __builtin_amdgcn_s_sleep(n); FENCE(); } while (0)
  if (hw == 0 && lane < 8) ldsv[L_FLAG + lane] = 0u;
  if (!EB0) BAR();
  if (wave < 8) {
    const size_t kvoff = ((size_t)h * SK + (SK - WIN) + s * 512 + wave * 64) * DH;
    const rsrc_t rk = RSRC(a.kc + kvoff, 64 * DH * 2), rv = RSRC(a.vc + kvoff, 64 * DH * 2);
    const u32 voff = k8 * 384 + c8 * 16;
    const u32 lv = (u32)(uintptr_t)(LAS float*)&lds[L_V + wave * VL * 768];
    u32x4 K[8][3], V[8 - VL][3];
    u32 pt0 = 0, pt1 = 0;
    if (PT) { pt0 = ld4nt(rk, lane < 6 && lane ? lane * 4096 : OOB, 0); pt1 = ld4nt(rv, lane < 6 ? lane * 4096 : OOB, 0); SB(); }
#pragma unroll
    for (int t = 0; t < 8 + KL; ++t)
#pragma unroll
      for (int j = 0; j < 3; ++j) {
        if (t < 8) K[t < 8 ? t : 0][j] = ld16nt(rk, voff + j * 128, t * 3072); else V[t < 8 ? 0 : t - 8][j] = ld16nt(rv, voff + j * 128, (VL + t - 8) * 3072);
        SB();
        if (t >= KL) asm volatile("" : "+v"(K[t >= KL ? t - KL : 0][j]));
        if (EB0 && t == KL - 1 && j == 2) BAR();
        SB();
      }
    if (PT) asm volatile("" ::"v"(pt0), "v"(pt1));
#pragma unroll
    for (int t = 0; t < VL; ++t)
#pragma unroll
      for (int j = 0; j < 3; ++j) {
        asm volatile("s_mov_b32 m0, %0\n\ts_nop 0\n\tbuffer_load_dwordx4 %1, %2, %3 offen offset:%4 " KVPOL " lds"
                     ::"s"(lv + (t * 3 + j) * 1024 - j * 128), "v"(voff), "s"(rv), "s"(t * 3072), "n"(j * 128) : "memory");
        VMW(PACE2);
      }
#pragma unroll
    for (int t = VL + KL; t < 8; ++t)
#pragma unroll
      for (int j = 0; j < 3; ++j) V[t - VL][j] = ld16nt(rv, voff + j * 128, t * 3072);
    SB();
    if (wave == WOW && lane == 0) ldsv[L_FLAG + 4] = 1u;
    T(0);
    LWAIT(ldsv[L_FLAG + 1] + ldsv[L_FLAG + 2] + ldsv[L_FLAG + 3] == 3u, 2);
    T(1);
    u32x4 q[3];
#pragma unroll
    for (int j = 0; j < 3; ++j) q[j] = *(const u32x4*)&lds[L_Q + j * 32 + c8 * 4];
    const u32 t32 = ldsu[L_TAG], t8 = t32 & 0xffu;
    float sc[8];
#pragma unroll
    for (int t = 0; t < 8; ++t) {
      float d0 = 0.f, d1 = 0.f;
#pragma unroll
      for (int j = 0; j < 3; ++j) {
        d0 = dot2(K[t][j][0], q[j][0], d0); d1 = dot2(K[t][j][1], q[j][1], d1);
        d0 = dot2(K[t][j][2], q[j][2], d0); d1 = dot2(K[t][j][3], q[j][3], d1);
      }
      float d = d0 + d1;
      d += dpp<XOR1>(d); d += dpp<XOR2>(d); d += dpp<HMIR>(d);
      sc[t] = rb(rb(d) * 0.07216878364870322f);
    }
    T(2);
    float mw = sc[0];
#pragma unroll
    for (int t = 1; t < 8; ++t) mw = fmaxf(mw, sc[t]);
    mw = fmaxf(mw, dpp<ROR8>(mw)); mw = mx16(mw); mw = mx32(mw);
    float lw = 0.f;
    u32x4 pb[8];
#pragma unroll
    for (int t = 0; t < 8; ++t) {
      const float ev = __expf(sc[t] - mw);
      const u32 pv = bfbits(ev) * 0x10001u;
      pb[t] = u32x4{pv & msk[0], pv & msk[1], pv & msk[2], pv & msk[3]};
      lw += ev;
      asm volatile("" : "+v"(pb[t]));
    }
    lw += dpp<ROR8>(lw); lw = sw16(lw, lw); lw = sw32(lw, lw);
    if (lane == 0) { lds[L_WM + wave] = mw; lds[L_WL + wave] = lw; }
    f32x4 acc[3] = {Z4, Z4, Z4};
#define PVM(w, t, j) acc[j] = __builtin_amdgcn_mfma_f32_16x16x32_bf16(__builtin_bit_cast(bf16x8, w), __builtin_bit_cast(bf16x8, pb[t]), acc[j], 0, 0, 0)
#define PVL(t) _Pragma("unroll") for (int j = 0; j < 3; ++j) PVM(*(const u32x4*)&lds[L_V + wave * VL * 768 + ((t) * 3 + j) * 256 + lane * 4], t, j)
    T(4);
    VMW(15 - 3 * KL);
    PVL(0); PVL(1); PVL(2);
    VMW(9 - 3 * KL);
    PVL(3); PVL(4);
#pragma unroll
    for (int t = VL; t < 8; ++t)
#pragma unroll
      for (int j = 0; j < 3; ++j) PVM(V[t - VL][j], t, j);
    T(5);
    if (((lane >> 5) & 1) == ((lane >> 3) & 1)) {
#pragma unroll
      for (int j = 0; j < 3; ++j) *(f32x4*)&lds[L_RED + (wave * 2 + (lane >> 5)) * 192 + j * 64 + c8 * 8 + ((lane >> 4) & 1) * 4] = acc[j];
    }
    if (is_gu && wave < 4) {
      if (!(GW & 4)) { BAR(); T(6); }
      const int row0 = (gi & 3) * 160 + (wave & 1) * 80;
      const rsrc_t rg = RSRC((wave < 2 ? a.wg : a.wu) + (size_t)row0 * FF + (gi >> 2) * 64, 79 * FF * 2 + 128);
      u32x4 w[10];
      if (GW & 1) LWAIT(ldsv[L_FLAG + 5] != 0u, 4);
      for (int i = 0; i < wave; ++i) __builtin_amdgcn_s_sleep(WSL / 8);
#pragma unroll
      for (int i = 0; i < 10; ++i) { SB(); if (i) __builtin_amdgcn_s_sleep(WSL / 2); w[i] = ld16nt(rg, k8 * (FF * 2) + c8 * 16, i * 8 * FF * 2); SB(); }
      if (GW & 4) { BAR(); T(6); }
      const u32 o = (lane & 40) == 0 ? WS_GU + ((gi >> 2) * 4 + (lane >> 4) * 2 + (c8 >> 2)) * 3072 + ((gi & 3) * 2 + (wave & 1)) * 128 + (wave >> 1) * 64 + (c8 & 3) * 16 : OOB;
      if (PRIME & 2) { const u32x4 p0 = ld16c(rws, o, 0), p1 = ld16c(rws, o, 2048); asm volatile("" ::"v"(p0), "v"(p1)); }
      VMW(0);
      BAR();
      T(9);
      u32 hv[10];
#pragma unroll
      for (int i = 0; i < 10; ++i) hv[i] = ldsu[L_X + (wave & 1) * 80 + i * 8 + k8];
      f32x4 g0 = Z4, g1 = Z4;
#pragma unroll
      for (int i = 0; i < 5; ++i) { g0 = rowmac(w[2 * i], hv[2 * i], msk, g0); g1 = rowmac(w[2 * i + 1], hv[2 * i + 1], msk, g1); }
      g0 = fold(g0 + g1);
      const u32x4 pk = gpack4(g0[0], g0[1], g0[2], g0[3], t8);
#pragma unroll
      for (int i = 0; i < 3; ++i) st16c(pk, rws, o, i * 1024);
      T(10);
    } else if (is_dn && wave < 3) {
      if (!(GW & 8)) { BAR(); T(6); }
      const int L = wave * 64 + lane;
      const rsrc_t rd = RSRC(a.wd + (size_t)((dj >> 2) * 64 + (dj & 2) * 16 + (dj & 1) * 4) * DM, 28 * DM * 2);
      u32 wp[8][4];
      {
        if (GW & 2) { LWAIT(ldsv[L_FLAG + 5] != 0u, 4); __builtin_amdgcn_s_sleep(DDN2); }
        u32x2 wd[16];
#pragma unroll
        for (int p = 0; p < 16; ++p) { SB(); __builtin_amdgcn_s_sleep(WSD); wd[p] = ld8nt(rd, L < 160 ? L * 8 : OOB, (8 * (p & 3) + (p >> 2)) * 1280); SB(); }
        if (GW & 8) { BAR(); T(6); LWAIT(ldsv[L_FLAG + 5] != 0u, 4); __builtin_amdgcn_s_sleep(DDN3); }
#pragma unroll
        for (int r = 0; r < 8; ++r) {
          pair4(wp[r], wd[(r >> 2) * 8 + (r & 3)], wd[(r >> 2) * 8 + 4 + (r & 3)]);
          asm volatile("" : "+v"(wp[r][0]), "+v"(wp[r][1]), "+v"(wp[r][2]), "+v"(wp[r][3]));
        }
      }
      const u32 o5 = L < 160 ? WS_G5 + (L / CH) * (96 * CH * 16) + (dj * CH + L % CH) * 16 : OOB;
      SB();
      if (PRIME & 2) { const u32x4 p0 = ld16c(rws, o5, 0); asm volatile("" ::"v"(p0)); }
      __builtin_amdgcn_s_sleep(DDN);
      const u32 og[1] = {WS_GU + dj * 3072 + wave * 1024 + lane * 16};
      u32x4 gg[1];
      gather<1, true, PS>(gg, rws, og, t8);
      const u32x4 g = gg[0];
      T(9);
      float v = rows4(gval(g[0]), gval(g[1]), gval(g[2]), gval(g[3]));
      v = rb(v + dpp<ROR8>(v));
      const u32 hb = bfbits(v * dpp<ROR4>(v));
      st16c(proj4<8>(wp, hb | (asu(dpp<XOR1>(asf(hb))) << 16), t8), rws, o5, 0);
      T(10);
    } else {
      BAR(); T(6);
      if (ri < NRED) {
        const int lo = L_X + (4 * (lane & 3) + (lane >> 4)) * 8 + wave;
        LWAIT(ldsv[L_FLAG + 5] != 0u, 2);
        {
          u32x4 g[2]; const u32 go[2] = {WS_G2 + ri * 16384 + wave * 2048 + lane * 16, WS_G2 + ri * 16384 + wave * 2048 + 1024 + lane * 16};
          gather<2, true, PS>(g, rws, go, t8);
          const float z = redrow(rows4(gval(g[0][0]) + gval(g[1][0]), gval(g[0][1]) + gval(g[1][1]), gval(g[0][2]) + gval(g[1][2]), gval(g[0][3]) + gval(g[1][3])));
          if ((lane & 12) == 0) lds[lo] = z;
        }
        BAR();
        if (wave < 6) {
          __builtin_amdgcn_s_sleep(DGU);
          u32x4 g[1]; const u32 go[1] = {WS_G5 + ri * 6144 + wave * 1024 + lane * 16};
          gather<1, true, PS>(g, rws, go, t8);
          const float z = redrow(rows4(gval(g[0][0]), gval(g[0][1]), gval(g[0][2]), gval(g[0][3])));
          if ((lane & 12) == 0) lds[lo + 128] = z;
          BAR();
        }
      }
    }
    if (wave == SW) TDUMP(16);
  } else if (wave == 8) {
    __builtin_amdgcn_s_setprio(3);
    T(0);
    const int mcol = b >> 2, mk = b & 3;
    const rsrc_t rc = RSRC(a.c, DM * 2), rx = RSRC(a.x, DM * 2);
    u32 xv[5];
    u32 cv[3];
#pragma unroll
    for (int i = 0; i < 3; ++i) cv[i] = ld2(rc, i == 2 && lane >= 32 ? OOB : (mk * 160 + lane) * 2, i * 128);
    u32x4 w[20];
    {
      const rsrc_t rm = RSRC(a.wm + (size_t)mk * 160 * 3840 + (mcol < 60 ? mcol : 0) * 64, mcol < 60 ? 159 * 7680 + 128 : 0);
#pragma unroll
      for (int i = 0; i < 20; ++i) w[i] = ld16nt(rm, k8 * 7680 + c8 * 16, i * 8 * 7680);
    }
    const u32x2 ew = ld8c(rws, WS_E, 0);
#pragma unroll
    for (int i = 0; i < 5; ++i) xv[i] = ld4nt(rx, lane * 4, i * 256);
    const u32 xs = ld4nt(rx, lane < 40 ? s * 160 + lane * 4 : OOB, 0);
    SB();
    if (EB0) BAR();
    T(1);
#pragma unroll
    for (int i = 0; i < 3; ++i) {
      const float c0 = blo(cv[i]);
      const u32 sv = bfbits(c0 / (1.f + __expf(-c0)));
      ldsu[L_X + i * 64 + lane] = sv | (sv << 16);
    }
    asm volatile("s_waitcnt lgkmcnt(0)" ::: "memory");
    T(2);
    u32 t32;
    {
      f32x4 g = Z4;
#pragma unroll
      for (int i = 0; i < 20; ++i) g = rowmac(w[i], ldsu[L_X + i * 8 + k8], msk, g);
      g = fold(g);
      t32 = __builtin_amdgcn_readfirstlane(ew[0] + 1u);
      const u32 o = (lane & 40) == 0 && mcol < 60 ? WS_MOD + (mk * 3840 + mcol * 64 + (lane >> 4) * 32 + c8) * 8 : OOB;
#pragma unroll
      for (int i = 0; i < 4; ++i) st8c(u32x2{asu(g[i]), t32}, rws, o + i * 64, 0);
    }
    const u32 t8 = t32 & 0xffu;
    T(3);
    {
      float sm = 0.f;
#pragma unroll
      for (int i = 0; i < 5; ++i) sm += blo(xv[i]) + bhi(xv[i]);
      const float m = wave_sum(sm) * (1.f / 640.f);
      float sv = 0.f;
#pragma unroll
      for (int i = 0; i < 5; ++i) { const float d0 = blo(xv[i]) - m, d1 = bhi(xv[i]) - m; sv += d0 * d0 + d1 * d1; }
      const float v = rb(wave_sum(sv) * (1.f / 640.f));
      const float mb = rb(m), den = rb(sqrtf(rb(v + 1e-5f)));
      const float n0 = rb(rb(blo(xs) - mb) / den), n1 = rb(rb(bhi(xs) - mb) / den);
      u32 go[8]; u32x4 gm[8];
#pragma unroll
      for (int i = 0; i < 8; ++i) go[i] = WS_MOD + ((i >> 1) * 3840 + (i & 1) * 640 + s * 80 + (lane < 40 ? lane : 0) * 2) * 8;
      __builtin_amdgcn_s_sleep(DMO);
      gather<8, false, 2>(gm, rws, go, t32);
      float sa0 = 0.f, sa1 = 0.f, ca0 = 0.f, ca1 = 0.f;
#pragma unroll
      for (int i = 0; i < 4; ++i) { sa0 += asf(gm[2 * i][0]); sa1 += asf(gm[2 * i][2]); ca0 += asf(gm[2 * i + 1][0]); ca1 += asf(gm[2 * i + 1][2]); }
      const u32 h0 = bfbits(rb(n0 * rb(1.f + rb(ca0))) + rb(sa0)), h1 = bfbits(rb(n1 * rb(1.f + rb(ca1))) + rb(sa1));
      if (lane < 40) *(LAS u32x2*)&ldsu[L_H + lane * 2] = u32x2{h0 | (h0 << 16), h1 | (h1 << 16)};
      if (lane == 0) ldsu[L_TAG] = t32;
      asm volatile("s_waitcnt lgkmcnt(0)" ::: "memory");
      if (lane == 0) ldsv[L_FLAG] = 1u;
    }
    T(4);
    const bool is_red = ri < NRED;
    const int rcol = ri * (4 * CH) + 4 * (lane % CH) + (lane >> 4);
    const bool rst = (lane & 15) < CH;
    float gav = 0.f, xrv = 0.f, gbv = 0.f;
    if (is_red) {
      xrv = blo(ld2(rx, rcol * 2, 0));
      float mv[4];
      for (u32 spins = 0;; ++spins) {
        FENCE();
        u32 bad = 0;
#pragma unroll
        for (int i = 0; i < 4; ++i) {
          const u32x2 g0 = ld8c(rws, WS_MOD + (((lane >> 2) & 3) * 3840 + 1280 + rcol) * 8, i * 640 * 8);
          mv[i] = asf(g0[0]); bad |= g0[1] ^ t32;
        }
        if (all_ok(bad) || spins > SPIN_CAP) break;
        __builtin_amdgcn_s_sleep(2);
      }
#pragma unroll
      for (int i = 0; i < 4; ++i) { mv[i] += dpp<ROR8>(mv[i]); mv[i] = rb(mv[i] + dpp<ROR4>(mv[i])); }
      gav = mv[0]; gbv = mv[3];
      st8c(u32x2{(bfbits(1.f + mv[2]) << 16) | bfbits(mv[1]), t32}, rws, rst ? WS_CS + rcol * 8 : OOB, 0);
    }
    u32 cs[4] = {0u, 0u, 0u, 0u};
    if (is_gu) {
      __builtin_amdgcn_s_sleep(127);
      for (u32 spins = 0;; ++spins) {
        FENCE();
        u32 bad = 0;
#pragma unroll
        for (int i = 0; i < 2; ++i) {
          const u32x4 g0 = ld16c(rws, WS_CS + ((gi & 3) * 160 + (i * 64 + lane < 80 ? i * 64 + lane : 0) * 2) * 8, 0);
          cs[2 * i] = g0[0]; cs[2 * i + 1] = g0[2];
          bad |= (g0[1] ^ t32) | (g0[3] ^ t32);
        }
        if (all_ok(bad) || spins > SPIN_CAP) break;
        __builtin_amdgcn_s_sleep(16);
      }
    }
    T(5);
    BAR();
    if (is_gu) LWAIT(ldsv[L_FLAG + 5] != 0u, 2);
    T(6);
    if (is_gu) {
      u32 xo[5];
#pragma unroll
      for (int i = 0; i < 5; ++i) xo[i] = WS_X1 + (gi % NXR) * X1S + (((gi & 3) * 160 + i * 128 + lane * 2) % 640) * 8;
      __builtin_amdgcn_s_sleep(DX1);
      u32x4 gx[5];
      gather<5, false, PS>(gx, rws, xo, t32);
      float xq[10];
#pragma unroll
      for (int i = 0; i < 5; ++i) { xq[2 * i] = asf(gx[i][0]); xq[2 * i + 1] = asf(gx[i][2]); }
      T(7);
      const float x0 = asf(__builtin_amdgcn_readfirstlane(asu(xq[0])));
      float s1 = 0.f, s2 = 0.f;
#pragma unroll
      for (int i = 0; i < 10; ++i) { const float d = xq[i] - x0; s1 += d; s2 = fmaf(d, d, s2); }
      float z = sw32(s1, s2);
      z += dpp<XOR1>(z); z += dpp<XOR2>(z); z += dpp<HMIR>(z); z += dpp<ROR8>(z); z = sw16(z, z) * (1.f / 640.f);
      s1 = asf(__builtin_amdgcn_readlane(asu(z), 0)); s2 = asf(__builtin_amdgcn_readlane(asu(z), 32));
      const float v = rb(fmaxf(fmaf(-s1, s1, s2), 0.f));
      const float den = rb(__builtin_amdgcn_sqrtf(rb(v + 1e-5f))), mb = rb(x0 + s1), rc1 = __builtin_amdgcn_rcpf(den);
      u32 hv[4];
#pragma unroll
      for (int i = 0; i < 4; ++i) hv[i] = bfbits(rb(rb(fdiv(rb(xq[i] - mb), den, rc1)) * bhi(cs[i])) + blo(cs[i])) * 0x10001u;
      *(LAS u32x2*)&ldsu[L_X + lane * 2] = u32x2{hv[0], hv[1]};
      *(LAS u32x2*)&ldsu[L_X + 128 + lane * 2] = u32x2{hv[2], hv[3]};
      BAR();
      T(8);
    } else if (is_red) {
      const int lo = L_X + (4 * (lane & 3) + (lane >> 4)) * 8;
      const u32 xo = WS_X1 + ((lane & 15) / CH) * X1S + rcol * 8;
      const rsrc_t ro = RSRC(a.out, DM * 2);
      BAR();
      T(7);
      const f32x4 a0 = *(const f32x4*)&lds[lo], a1 = *(const f32x4*)&lds[lo + 4];
      const float x1 = rb(xrv + rb(gav * rb(((a0[0] + a0[1]) + (a0[2] + a0[3])) + ((a1[0] + a1[1]) + (a1[2] + a1[3])))));
#pragma unroll
      for (int i = 0; i < XR; ++i) st8c(u32x2{asu(x1), t32}, rws, xo, i * (16 / CH) * X1S);
      T(8);
      const u32 po = ld2(ro, rcol * 2, 0);
      asm volatile("" ::"v"(po));
      BAR();
      T(9);
      const f32x4 d0 = *(const f32x4*)&lds[lo + 128]; const f32x2 d1 = *(const f32x2*)&lds[lo + 132];
      const float dn = ((d0[0] + d0[1]) + (d0[2] + d0[3])) + (d1[0] + d1[1]);
      __builtin_amdgcn_raw_buffer_store_b16((u16)bfbits(x1 + rb(gbv * rb(dn))), ro, rst ? rcol * 2 : OOB, 0, 0);
      if (ri == 0 && lane == 0) st8c(u32x2{t32, t32}, rws, WS_E, 0);
      T(10);
    }
    TDUMP(0);
  } else {
    __builtin_amdgcn_s_setprio(3);
    const int j = wave - 9;
    u32 t32;
    {
      const rsrc_t rq = RSRC(a.wq + (size_t)s * 80 * (NH * DH) + h * DH + j * 64, 79 * NH * DH * 2 + 128);
      u32x4 w[10];
#pragma unroll
      for (int t = 0; t < 10; ++t) w[t] = ld16nt(rq, k8 * (NH * DH * 2) + c8 * 16, t * 8 * NH * DH * 2);
      SB();
      if (EB0) BAR();
      LWAIT(ldsv[L_FLAG] != 0u, 2);
      T(0);
      t32 = ldsu[L_TAG];
      f32x4 g = Z4;
#pragma unroll
      for (int t = 0; t < 10; ++t) g = rowmac(w[t], ldsu[L_H + t * 8 + k8], msk, g);
      g = fold(g);
      const u32 o = (lane & 40) == 0 ? WS_QP + ((h * 8 + s) * 192 + j * 64 + (lane >> 4) * 32 + c8) * 8 : OOB;
#pragma unroll
      for (int i = 0; i < 4; ++i) st8c(u32x2{asu(g[i]), t32}, rws, o + i * 64, 0);
    }
    const u32 t8 = t32 & 0xffu;
    T(1);
    float q0 = 0.f, q1 = 0.f;
    {
      u32x4 gq[4]; u32 go[4];
#pragma unroll
      for (int i = 0; i < 4; ++i) go[i] = WS_QP + ((h * 8 + i * 2 + (lane >> 5)) * 192 + j * 64 + (lane & 31) * 2) * 8;
      __builtin_amdgcn_s_sleep(DQ);
      gather<4, false, 2>(gq, rws, go, t32);
#pragma unroll
      for (int i = 0; i < 4; ++i) { q0 += asf(gq[i][0]); q1 += asf(gq[i][2]); }
    }
    q0 = sw32(q0, q0); q1 = sw32(q1, q1);
    if (lane < 32) ldsu[L_Q + j * 32 + lane] = bfbits(q0) | (bfbits(q1) << 16);
    asm volatile("s_waitcnt lgkmcnt(0)" ::: "memory");
    if (lane == 0) ldsv[L_FLAG + 1 + j] = 1u;
    T(2);
    LWAIT(ldsv[L_FLAG + 4] != 0u, 4);
    const int L = j * 64 + lane;
    u32x2 wo[24];
    const rsrc_t rw = RSRC(a.wo + (size_t)h * DH * DM, DH * DM * 2);
#define WOLD(r) do { const int p_ = s * 24 + (r); wo[r] = ld8nt(rw, L < 160 ? L * 8 : OOB, ((p_ & ~63) + 8 * (p_ & 7) + ((p_ & 63) >> 3)) * 1280); } while (0)
#pragma unroll
    for (int r = 0; r < WOE; ++r) WOLD(r);
    const int sp = L / 24;
    const bool isml = (lane & 3) == 3;
    const u32 o1 = !isml ? WS_G1 + (h * 8 + sp) * 3072 + (lane & 3) * 1024 + s * 128 + ((L & ~3) - sp * 24) * 4
                         : (lane < 32 ? WS_G1 + (h * 8 + (lane >> 2)) * 3072 + j * 1024 + s * 128 + 96 : OOB);
    const u32 o2 = L < 160 ? WS_G2 + (L / CH) * (4096 * CH) + ((h * 8 + s) * CH + L % CH) * 16 : OOB;
    u32x4 p1, p2;
    if (PRIME) { p1 = ld16c(rws, o1, 0); p2 = ld16c(rws, o2, 0); }
    BAR();
    {
      const f32x4 m0 = *(const f32x4*)&lds[L_WM], m1 = *(const f32x4*)&lds[L_WM + 4];
      const f32x4 l0 = *(const f32x4*)&lds[L_WL], l1 = *(const f32x4*)&lds[L_WL + 4];
      float ml[2] = {fmaxf(fmaxf(fmaxf(m0[0], m0[1]), fmaxf(m0[2], m0[3])), fmaxf(fmaxf(m1[0], m1[1]), fmaxf(m1[2], m1[3]))), 0.f};
      float v = 0.f;
#pragma unroll
      for (int w8 = 0; w8 < 8; ++w8) {
        const float e = __expf((w8 < 4 ? m0[w8 & 3] : m1[w8 & 3]) - ml[0]);
        v = fmaf(e, lds[L_RED + w8 * 384 + L] + lds[L_RED + w8 * 384 + 192 + L], v);
        ml[1] = fmaf(e, w8 < 4 ? l0[w8 & 3] : l1[w8 & 3], ml[1]);
      }
      const float v0 = dpp<0x00>(v), v1 = dpp<0x55>(v), v2 = dpp<0xAA>(v), v3 = dpp<0xFF>(v);
      st16c(gpack4(isml ? ml[0] : v0, isml ? ml[1] : v1, v2, v3, t8), rws, o1, 0);
    }
    T(3);
    const u32 og[1] = {WS_G1 + (h * 8 + s) * 3072 + j * 1024 + (c8 == 7 ? lane - 1 : lane) * 16};
    SB();
#pragma unroll
    for (int r = WOE; r < 24; ++r) WOLD(r);
    SB();
    __builtin_amdgcn_s_sleep(DE1);
    u32x4 gg[1] = {ld16c(rws, og[0], 0)};
    SB();
    u32 wp[12][4];
#pragma unroll
    for (int r = 0; r < 12; ++r) {
      pair4(wp[r], wo[(r >> 2) * 8 + (r & 3)], wo[(r >> 2) * 8 + 4 + (r & 3)]);
      asm volatile("" : "+v"(wp[r][0]), "+v"(wp[r][1]), "+v"(wp[r][2]), "+v"(wp[r][3]));
    }
    SB();
    T(4);
    if (PRIME) asm volatile("" ::"v"(p1), "v"(p2));
    polln<1, true, PS>(gg, rws, og, t8);
    const u32x4 g = gg[0];
    T(5);
    {
      const float g0 = gval(g[0]), g1 = gval(g[1]);
      const float M = mx32(mx16(fmaxf(g0, dpp<ROR8>(g0))));
      const float ev = __expf(g0 - M);
      float Ls = ev * g1; Ls += dpp<ROR8>(Ls); Ls = sw16(Ls, Ls); Ls = sw32(Ls, Ls);
      const float w1 = dpp<0xAA>(ev * __builtin_amdgcn_rcpf(Ls)), w2 = dpp<HMIR>(w1), ws = (c8 & 4) ? w1 : w2;
      float o = rows4(g0 * ws, g1 * ws, gval(g[2]) * ws, gval(g[3]) * ws);
      o += dpp<ROR8>(o);
      const u32 ob = bfbits(o);
      st16c(proj4<12>(wp, ob | (asu(dpp<XOR1>(asf(ob))) << 16), t8), rws, o2, 0);
    }
    if (j == 0 && lane == 0) ldsv[L_FLAG + 5] = 1u;
    T(6);
    if (wave == 9) TDUMP(32);
  }
}

extern "C" void kernel_launch(void* const* d_in, const int* in_sizes, int n_in, void* d_out, int out_size, void* d_ws,
                              size_t ws_size, hipStream_t stream) {
  Args a;
  a.x = (const u16*)d_in[0]; a.kc = (const u16*)d_in[1]; a.vc = (const u16*)d_in[2]; a.wq = (const u16*)d_in[3];
  a.wo = (const u16*)d_in[4]; a.wg = (const u16*)d_in[6]; a.wu = (const u16*)d_in[7]; a.wd = (const u16*)d_in[8];
  a.c = (const u16*)d_in[9]; a.wm = (const u16*)d_in[10];
  a.out = (u16*)d_out; a.ws = (unsigned char*)d_ws;
  fused_kernel<<<NWG, NTHR, 0, stream>>>(a);
}
```
